```python
import torch

def _softmax(x, axis=-1):
    return torch.softmax(x, dim=axis)
def _gelu(x):
    return torch.nn.functional.gelu(x, approximate="tanh")
def _silu(x):
    return torch.nn.functional.silu(x)
def _sigmoid(x):
    return torch.sigmoid(x)
def _layernorm(x, axis=-1, eps=1e-5):
    m = x.mean(dim=axis, keepdim=True)
    v = x.var(dim=axis, keepdim=True, unbiased=False)
    return (x - m) / torch.sqrt(v + eps)
def _scatter_add(tgt, idx, src, axis=0):
    return tgt.clone().index_add_(axis, idx, src)
def _segment_sum(src, seg, n):
    out = torch.zeros((n,) + src.shape[1:], dtype=src.dtype)
    return out.index_add_(0, seg, src)
def _pad(x, pw):
    flat = tuple(v for pair in reversed(pw) for v in pair)
    return torch.nn.functional.pad(x, flat)

kernel_name = 'recurrent_scan__ssm_conv_s5__glu_gated_bilinear__prompt'

def setup_inputs():

    _w = (lambda a: (lambda t: t.to(torch.bfloat16) if t.is_floating_point() else t)(torch.as_tensor(a)))
    def _A_setup():
        B, S, D = 1, 2048, 512
        P = D
        Lambda_re = -0.5 * (lambda _x: torch.exp(torch.as_tensor(_x)))((lambda *s: torch.randn(*s, dtype=torch.bfloat16) * 0.8)(P))
        Lambda_im = (lambda *s: torch.randn(*s, dtype=torch.bfloat16) * 0.8)(P) * 1.0
        B_re = (lambda *s: torch.randn(*s, dtype=torch.bfloat16) * 1.6)(P, D) * (D ** -0.5)
        B_im = (lambda *s: torch.randn(*s, dtype=torch.bfloat16) * 0.8)(P, D) * (D ** -0.5)
        C_re = (lambda *s: torch.randn(*s, dtype=torch.bfloat16) * 1.6)(D, P) * (P ** -0.5)
        C_im = (lambda *s: torch.randn(*s, dtype=torch.bfloat16) * 0.8)(D, P) * (P ** -0.5)
        log_dt = (lambda *s: torch.randn(*s, dtype=torch.bfloat16) * 0.8)(P) * 0.3 - 2.0
        Dskip = (lambda *s: torch.randn(*s, dtype=torch.bfloat16) * 0.8)(D)
        return {
            "x": _w((lambda *s: torch.randn(*s, dtype=torch.bfloat16) * 0.05)(B, S, D)),
            "Lambda_re": _w(Lambda_re),
            "Lambda_im": _w(Lambda_im),
            "B_re": _w(B_re),
            "B_im": _w(B_im),
            "C_re": _w(C_re),
            "C_im": _w(C_im),
            "log_dt": _w(log_dt),
            "Dskip": _w(Dskip),
        }
    _A_in = _A_setup()

    def _B_setup():
        B, S, D, F = 1, 2048, 512, 1792
        return {
            "x": _w((lambda *s: torch.randn(*s, dtype=torch.bfloat16) * 0.05)(B, S, D)),
            "w_gate": _w((lambda *s: torch.randn(*s, dtype=torch.bfloat16) * 0.2)(D, F) * (D ** -0.5)),
            "w_up": _w((lambda *s: torch.randn(*s, dtype=torch.bfloat16) * 0.2)(D, F) * (D ** -0.5)),
            "w_down": _w((lambda *s: torch.randn(*s, dtype=torch.bfloat16) * 0.2)(F, D) * (F ** -0.5)),
        }
    _B_in = _B_setup()

    return {
        "A_x": _A_in["x"],
        "A_Lambda_re": _A_in["Lambda_re"],
        "A_Lambda_im": _A_in["Lambda_im"],
        "A_B_re": _A_in["B_re"],
        "A_B_im": _A_in["B_im"],
        "A_C_re": _A_in["C_re"],
        "A_C_im": _A_in["C_im"],
        "A_log_dt": _A_in["log_dt"],
        "A_Dskip": _A_in["Dskip"],
        "B_x": _B_in["x"],
        "B_w_gate": _B_in["w_gate"],
        "B_w_up": _B_in["w_up"],
        "B_w_down": _B_in["w_down"],
    }

def reference(A_x, A_Lambda_re, A_Lambda_im, A_B_re, A_B_im, A_C_re, A_C_im, A_log_dt, A_Dskip, B_x, B_w_gate, B_w_up, B_w_down):
    def _A_ref(x, Lambda_re, Lambda_im, B_re, B_im, C_re, C_im, log_dt, Dskip):
        Bsz, L, H = x.shape
        Lam = Lambda_re + 1j * Lambda_im
        Bmat = B_re + 1j * B_im
        Cmat = C_re + 1j * C_im
        dt = torch.exp(log_dt)
        Lam_bar = torch.exp(dt * Lam)
        B_bar = ((Lam_bar - 1.0) / Lam)[:, None] * Bmat
        Bu = torch.einsum("bld,pd->blp", x + 0j, B_bar)
        h = torch.zeros_like(Bu[:, 0, :])
        hs = []
        for t in range(L):
            h = Lam_bar[None, :] * h + Bu[:, t, :]
            hs.append(h)
        hseq = torch.stack(hs, 1)
        y = 2.0 * torch.einsum("blp,dp->bld", hseq, Cmat).real + x * Dskip[None, None, :]
        return y

    def _B_ref(x, w_gate, w_up, w_down):
        g = torch.einsum("bsd,df->bsf", x, w_gate)
        u = torch.einsum("bsd,df->bsf", x, w_up)
        h = g * u
        return torch.einsum("bsf,fd->bsd", h, w_down)

    x = A_x
    xn = _layernorm(x, axis=-1)
    s = _A_ref(xn, A_Lambda_re, A_Lambda_im, A_B_re, A_B_im, A_C_re, A_C_im, A_log_dt, A_Dskip)
    g = _silu(_B_ref(xn, B_w_gate, B_w_up, B_w_down))
    return x + s * g
```

```cpp
#include <hip/hip_runtime.h>
#include <hip/hip_bf16.h>
#include <stdint.h>

#define L 2048
#define D 512
#define P 512
#define F 1792

__device__ __forceinline__ float bf2f(unsigned short v) { return __uint_as_float(((unsigned)v) << 16); }
__device__ __forceinline__ unsigned short f2bf(float f) {
    unsigned u = __float_as_uint(f);
    u += 0x7fffu + ((u >> 16) & 1u);
    return (unsigned short)(u >> 16);
}
__device__ __forceinline__ float rbf(float f) { return bf2f(f2bf(f)); }

__global__ void ln_k(const unsigned short* x, unsigned short* xn) {
    int t = blockIdx.x; int tid = threadIdx.x;
    float v[8]; float s = 0;
    for (int i = 0; i < 8; i++) { v[i] = bf2f(x[t * D + tid * 8 + i]); s += v[i]; }
    for (int o = 32; o > 0; o >>= 1) s += __shfl_xor(s, o);
    float mean = s / D;
    float q = 0;
    for (int i = 0; i < 8; i++) { float d = v[i] - mean; q += d * d; }
    for (int o = 32; o > 0; o >>= 1) q += __shfl_xor(q, o);
    float var = q / D;
    float mb = rbf(mean), vb = rbf(var);
    float den = rbf(sqrtf(rbf(vb + 1e-5f)));
    for (int i = 0; i < 8; i++) xn[t * D + tid * 8 + i] = f2bf(rbf(v[i] - mb) / den);
}

__global__ void bu_k(const unsigned short* xn, const unsigned short* Bre, const unsigned short* Bim,
                     const unsigned short* Lre, const unsigned short* Lim, const unsigned short* logdt, float2* Bu) {
    int t = blockIdx.x; int p = threadIdx.x;
    float ar = 0, ai = 0;
    for (int d = 0; d < D; d++) { float xv = bf2f(xn[t * D + d]); ar += xv * bf2f(Bre[p * D + d]); ai += xv * bf2f(Bim[p * D + d]); }
    float dt = rbf(expf(bf2f(logdt[p])));
    float lr = bf2f(Lre[p]), li = bf2f(Lim[p]);
    float er = expf(dt * lr); float s, c; sincosf(dt * li, &s, &c);
    float br = er * c - 1.0f, bi = er * s;
    float den = lr * lr + li * li;
    float cr = (br * lr + bi * li) / den, ci = (bi * lr - br * li) / den;
    Bu[t * P + p] = make_float2(cr * ar - ci * ai, cr * ai + ci * ar);
}

__global__ void scan_k(float2* Bu, const unsigned short* Lre, const unsigned short* Lim, const unsigned short* logdt) {
    int p = threadIdx.x + blockIdx.x * blockDim.x;
    float dt = rbf(expf(bf2f(logdt[p])));
    float lr = bf2f(Lre[p]), li = bf2f(Lim[p]);
    float er = expf(dt * lr); float s, c; sincosf(dt * li, &s, &c);
    float lbr = er * c, lbi = er * s;
    float hr = 0, hi = 0;
    for (int t = 0; t < L; t++) {
        float2 b = Bu[t * P + p];
        float nr = lbr * hr - lbi * hi + b.x, ni = lbr * hi + lbi * hr + b.y;
        hr = nr; hi = ni;
        Bu[t * P + p] = make_float2(hr, hi);
    }
}

__global__ void glu_k(const unsigned short* xn, const unsigned short* wg, const unsigned short* wu, unsigned short* h) {
    int t = blockIdx.x;
    for (int f = threadIdx.x; f < F; f += blockDim.x) {
        float g = 0, u = 0;
        for (int d = 0; d < D; d++) { float xv = bf2f(xn[t * D + d]); g += xv * bf2f(wg[d * F + f]); u += xv * bf2f(wu[d * F + f]); }
        h[t * F + f] = f2bf(rbf(g) * rbf(u));
    }
}

__global__ void fin_k(const unsigned short* x, const unsigned short* xn, const float2* hs, const unsigned short* h,
                      const unsigned short* Cre, const unsigned short* Cim, const unsigned short* wd, const unsigned short* Dsk, float* out) {
    int t = blockIdx.x; int d = threadIdx.x;
    float y = 0;
    for (int p = 0; p < P; p++) { float2 hv = hs[t * P + p]; y += hv.x * bf2f(Cre[d * P + p]) - hv.y * bf2f(Cim[d * P + p]); }
    float dn = 0;
    for (int f = 0; f < F; f++) dn += bf2f(h[t * F + f]) * bf2f(wd[f * D + d]);
    dn = rbf(dn);
    float g = rbf(dn / (1.0f + expf(-dn)));
    float s = 2.0f * y + rbf(bf2f(xn[t * D + d]) * bf2f(Dsk[d]));
    out[t * D + d] = bf2f(x[t * D + d]) + s * g;
}

extern "C" void kernel_launch(void* const* d_in, const int* in_sizes, int n_in,
                              void* d_out, int out_size, void* d_ws, size_t ws_size,
                              hipStream_t stream) {
    const unsigned short* x = (const unsigned short*)d_in[0];
    const unsigned short* Lre = (const unsigned short*)d_in[1];
    const unsigned short* Lim = (const unsigned short*)d_in[2];
    const unsigned short* Bre = (const unsigned short*)d_in[3];
    const unsigned short* Bim = (const unsigned short*)d_in[4];
    const unsigned short* Cre = (const unsigned short*)d_in[5];
    const unsigned short* Cim = (const unsigned short*)d_in[6];
    const unsigned short* logdt = (const unsigned short*)d_in[7];
    const unsigned short* Dsk = (const unsigned short*)d_in[8];
    const unsigned short* wg = (const unsigned short*)d_in[10];
    const unsigned short* wu = (const unsigned short*)d_in[11];
    const unsigned short* wd = (const unsigned short*)d_in[12];
    char* ws = (char*)d_ws;
    unsigned short* xn = (unsigned short*)ws;
    float2* Bu = (float2*)(ws + (4 << 20));
    unsigned short* h = (unsigned short*)(ws + (16 << 20));
    ln_k<<<L, 64, 0, stream>>>(x, xn);
    bu_k<<<L, P, 0, stream>>>(xn, Bre, Bim, Lre, Lim, logdt, Bu);
    scan_k<<<8, 64, 0, stream>>>(Bu, Lre, Lim, logdt);
    glu_k<<<L, 256, 0, stream>>>(xn, wg, wu, h);
    fin_k<<<L, D, 0, stream>>>(x, xn, Bu, h, Cre, Cim, wd, Dsk, (float*)d_out);
}
```

```cpp
#include <hip/hip_runtime.h>
#include <hip/hip_bf16.h>
#include <stdint.h>

#define L 2048
#define D 512
#define P 512
#define F 1792
typedef unsigned short u16;
typedef short s16x8 __attribute__((ext_vector_type(8)));
typedef short s16x4 __attribute__((ext_vector_type(4)));
typedef float f32x16 __attribute__((ext_vector_type(16)));
typedef unsigned u32x4 __attribute__((ext_vector_type(4)));
typedef __attribute__((address_space(3))) s16x4* lds_s16x4_p;

__device__ __forceinline__ float bf2f(u16 v) { return __uint_as_float(((unsigned)v) << 16); }
__device__ __forceinline__ u16 f2bf(float f) {
    unsigned u = __float_as_uint(f);
    u += 0x7fffu + ((u >> 16) & 1u);
    return (u16)(u >> 16);
}
__device__ __forceinline__ float rbf(float f) { return bf2f(f2bf(f)); }

struct LamInfo { float lbr, lbi, cr, ci; };
__device__ __forceinline__ void lam_bar(const u16* Lre, const u16* Lim, const u16* logdt, int p, float mult,
                                        float& pr, float& pi) {
    float dt = rbf(expf(bf2f(logdt[p])));
    float lr = bf2f(Lre[p]), li = bf2f(Lim[p]);
    float er = expf(mult * (dt * lr)); float s, c; sincosf(mult * (dt * li), &s, &c);
    pr = er * c; pi = er * s;
}

#define MFMA32(a, b, c) __builtin_amdgcn_mfma_f32_32x32x16_bf16(a, b, c, 0, 0, 0)

constexpr int A_BYTES = 128 * 1024;
constexpr int BROW = 704;
constexpr int GDEPTH = 6;
constexpr int SDEPTH = 3;

__global__ __launch_bounds__(256, 1) void k1(const u16* __restrict__ x, const u16* __restrict__ Lre, const u16* __restrict__ Lim,
        const u16* __restrict__ Bre, const u16* __restrict__ Bim, const u16* __restrict__ logdt,
        const u16* __restrict__ wg, const u16* __restrict__ wu,
        u16* __restrict__ xn_out, u16* __restrict__ h_out, float2* __restrict__ hloc, float2* __restrict__ Ec) {
    __shared__ __attribute__((aligned(16))) char smem[163840];
    const int tid = threadIdx.x, lane = tid & 63, wid = tid >> 6;
    const int b = blockIdx.x, idx = b >> 3;
    const int slot = 2 * (b & 7) + (idx & 1), rb = idx >> 1;
    const int r0 = rb * 128;
    const int wr = wid >> 1, wc = wid & 1;
    char* Bs = smem + A_BYTES;

    const bool is_ssm = slot < 4;
    u32x4 ring[GDEPTH][3];
    const u16* gsrc[4]; int ldst[4];
    int f0 = 0, p0 = 0;
    if (!is_ssm) {
        int g = slot - 4; f0 = min(g * 160, F - 160);
#pragma unroll
        for (int i = 0; i < 3; i++) {
            int c = tid + 256 * i; if (c >= 640) c = tid;
            int k = c / 40, cc = c % 40;
            int unit = cc >> 2, part = (cc >> 1) & 1, half = cc & 1;
            gsrc[i] = (part ? wu : wg) + k * F + f0 + 16 * unit + 8 * half;
            ldst[i] = k * BROW + cc * 16;
        }
#pragma unroll
        for (int s = 0; s < GDEPTH; s++)
#pragma unroll
            for (int i = 0; i < 3; i++) ring[s][i] = *(const u32x4*)(gsrc[i] + s * 16 * F);
    } else {
        p0 = slot * 128;
#pragma unroll
        for (int i = 0; i < 4; i++) {
            int c = tid + 256 * i; int n = c >> 2, ch = c & 3;
            gsrc[i] = (n < 128 ? Bre : Bim) + (p0 + (n & 127)) * D + ch * 8;
            ldst[i] = n * 64 + ((ch ^ ((n >> 2) & 3)) << 4);
        }
    }
    u32x4 sring[SDEPTH][4];
    if (is_ssm) {
#pragma unroll
        for (int s = 0; s < SDEPTH; s++)
#pragma unroll
            for (int i = 0; i < 4; i++) sring[s][i] = *(const u32x4*)(gsrc[i] + s * 32);
    }

#pragma unroll
    for (int half = 0; half < 2; half++) {
        u32x4 xv[16];
#pragma unroll
        for (int i = 0; i < 16; i++) xv[i] = *(const u32x4*)(x + (size_t)(r0 + wid * 32 + half * 16 + i) * D + lane * 8);
#pragma unroll
        for (int i = 0; i < 16; i++) {
            int row = wid * 32 + half * 16 + i;
            float v[8];
#pragma unroll
            for (int e = 0; e < 4; e++) { v[2 * e] = __uint_as_float(xv[i][e] << 16); v[2 * e + 1] = __uint_as_float(xv[i][e] & 0xffff0000u); }
            float s = 0;
#pragma unroll
            for (int e = 0; e < 8; e++) s += v[e];
#pragma unroll
            for (int o = 32; o > 0; o >>= 1) s += __shfl_xor(s, o);
            float mean = s * (1.0f / D);
            float q = 0;
#pragma unroll
            for (int e = 0; e < 8; e++) { float d = v[e] - mean; q += d * d; }
#pragma unroll
            for (int o = 32; o > 0; o >>= 1) q += __shfl_xor(q, o);
            float mb = rbf(mean), vb = rbf(q * (1.0f / D));
            float den = rbf(sqrtf(rbf(vb + 1e-5f)));
            u32x4 o4;
#pragma unroll
            for (int e = 0; e < 4; e++) {
                u16 lo = f2bf(rbf(v[2 * e] - mb) / den), hi = f2bf(rbf(v[2 * e + 1] - mb) / den);
                o4[e] = (unsigned)lo | ((unsigned)hi << 16);
            }
            *(u32x4*)(smem + row * 1024 + (((lane & 48) | ((lane ^ row) & 15)) << 4)) = o4;
            if (slot == 4) *(u32x4*)(xn_out + (size_t)(r0 + row) * D + lane * 8) = o4;
        }
    }

    const int l31 = lane & 31, lh = lane >> 5;
    if (!is_ssm) {
        f32x16 acc[2][5];
#pragma unroll
        for (int m = 0; m < 2; m++)
#pragma unroll
            for (int n = 0; n < 5; n++)
#pragma unroll
                for (int e = 0; e < 16; e++) acc[m][n][e] = 0.f;
#pragma unroll
        for (int i = 0; i < 3; i++) *(u32x4*)(Bs + ldst[i]) = ring[0][i];
#pragma unroll
        for (int i = 0; i < 3; i++) ring[0][i] = *(const u32x4*)(gsrc[i] + GDEPTH * 16 * F);
        __syncthreads();
        const unsigned trb = (unsigned)(size_t)Bs + (8 * lh + ((lane & 15) >> 2)) * BROW + (wc * 160 + 16 * ((lane >> 4) & 1) + 4 * (lane & 3)) * 2;
#pragma unroll
        for (int s = 0; s < 32; s++) {
            const int cur = s & 1;
            if (s + 1 < 32) {
#pragma unroll
                for (int i = 0; i < 3; i++) *(u32x4*)(Bs + (cur ^ 1) * (16 * BROW) + ldst[i]) = ring[(s + 1) % GDEPTH][i];
                if (s + 1 + GDEPTH < 32) {
#pragma unroll
                    for (int i = 0; i < 3; i++) ring[(s + 1) % GDEPTH][i] = *(const u32x4*)(gsrc[i] + (s + 1 + GDEPTH) * 16 * F);
                }
            }
            s16x8 af[2];
#pragma unroll
            for (int m = 0; m < 2; m++) {
                int row = wr * 64 + m * 32 + l31; int ch = 2 * s + lh;
                af[m] = *(const s16x8*)(smem + row * 1024 + (((ch & 48) | ((ch ^ row) & 15)) << 4));
            }
#pragma unroll
            for (int n = 0; n < 5; n++) {
                s16x4 b0 = __builtin_amdgcn_ds_read_tr16_b64_v4i16((lds_s16x4_p)(trb + cur * (16 * BROW) + n * 64));
                s16x4 b1 = __builtin_amdgcn_ds_read_tr16_b64_v4i16((lds_s16x4_p)(trb + cur * (16 * BROW) + n * 64 + 4 * BROW));
                s16x8 bf = __builtin_shufflevector(b0, b1, 0, 1, 2, 3, 4, 5, 6, 7);
#pragma unroll
                for (int m = 0; m < 2; m++) acc[m][n] = MFMA32(af[m], bf, acc[m][n]);
            }
            __syncthreads();
        }
#pragma unroll
        for (int m = 0; m < 2; m++)
#pragma unroll
            for (int n = 0; n < 5; n++)
#pragma unroll
                for (int e = 0; e < 16; e++) {
                    float mine = rbf(acc[m][n][e]);
                    float other = __shfl_xor(mine, 16);
                    if (!(lane & 16)) {
                        int row = wr * 64 + m * 32 + (e & 3) + 8 * (e >> 2) + 4 * lh;
                        h_out[(size_t)(r0 + row) * F + f0 + (wc * 5 + n) * 16 + (lane & 15)] = f2bf(mine * other);
                    }
                }
    } else {
        f32x16 acc[2][4];
#pragma unroll
        for (int m = 0; m < 2; m++)
#pragma unroll
            for (int n = 0; n < 4; n++)
#pragma unroll
                for (int e = 0; e < 16; e++) acc[m][n][e] = 0.f;
#pragma unroll
        for (int i = 0; i < 4; i++) *(u32x4*)(Bs + ldst[i]) = sring[0][i];
#pragma unroll
        for (int i = 0; i < 4; i++) sring[0][i] = *(const u32x4*)(gsrc[i] + SDEPTH * 32);
        __syncthreads();
#pragma unroll
        for (int s = 0; s < 16; s++) {
            const int cur = s & 1;
            if (s + 1 < 16) {
#pragma unroll
                for (int i = 0; i < 4; i++) *(u32x4*)(Bs + (cur ^ 1) * 16384 + ldst[i]) = sring[(s + 1) % SDEPTH][i];
                if (s + 1 + SDEPTH < 16) {
#pragma unroll
                    for (int i = 0; i < 4; i++) sring[(s + 1) % SDEPTH][i] = *(const u32x4*)(gsrc[i] + (s + 1 + SDEPTH) * 32);
                }
            }
#pragma unroll
            for (int kk = 0; kk < 2; kk++) {
                s16x8 af[2];
#pragma unroll
                for (int m = 0; m < 2; m++) {
                    int row = wr * 64 + m * 32 + l31; int ch = 4 * s + 2 * kk + lh;
                    af[m] = *(const s16x8*)(smem + row * 1024 + (((ch & 48) | ((ch ^ row) & 15)) << 4));
                }
#pragma unroll
                for (int n = 0; n < 4; n++) {
                    int nn = (n >> 1) * 128 + wc * 64 + (n & 1) * 32 + l31; int ch = 2 * kk + lh;
                    s16x8 bf = *(const s16x8*)(Bs + cur * 16384 + nn * 64 + ((ch ^ ((nn >> 2) & 3)) << 4));
#pragma unroll
                    for (int m = 0; m < 2; m++) acc[m][n] = MFMA32(af[m], bf, acc[m][n]);
                }
            }
            __syncthreads();
        }
        float2* bu = (float2*)smem;
#pragma unroll
        for (int j = 0; j < 2; j++) {
            int pl = wc * 64 + j * 32 + l31; int p = p0 + pl;
            float dt = rbf(expf(bf2f(logdt[p])));
            float lr = bf2f(Lre[p]), li = bf2f(Lim[p]);
            float er = expf(dt * lr); float sn, cs; sincosf(dt * li, &sn, &cs);
            float br = er * cs - 1.0f, bi = er * sn;
            float dn = lr * lr + li * li;
            float cr = (br * lr + bi * li) / dn, ci = (bi * lr - br * li) / dn;
#pragma unroll
            for (int m = 0; m < 2; m++)
#pragma unroll
                for (int e = 0; e < 16; e++) {
                    int row = wr * 64 + m * 32 + (e & 3) + 8 * (e >> 2) + 4 * lh;
                    float ar = acc[m][j][e], ai = acc[m][2 + j][e];
                    bu[row * 128 + pl] = make_float2(cr * ar - ci * ai, cr * ai + ci * ar);
                }
        }
        __syncthreads();
        const int pl = tid & 127, hf = tid >> 7, p = p0 + pl;
        float lbr, lbi; lam_bar(Lre, Lim, logdt, p, 1.0f, lbr, lbi);
        float hr = 0, hi = 0;
#pragma unroll 8
        for (int t = 0; t < 64; t++) {
            float2 v = bu[(hf * 64 + t) * 128 + pl];
            float nr = lbr * hr - lbi * hi + v.x, ni = lbr * hi + lbi * hr + v.y;
            hr = nr; hi = ni;
            if (hf == 0) hloc[(size_t)(r0 + t) * P + p] = make_float2(hr, hi);
            else bu[(64 + t) * 128 + pl] = make_float2(hr, hi);
        }
        float2* endst = (float2*)Bs;
        if (hf == 0) endst[pl] = make_float2(hr, hi);
        __syncthreads();
        if (hf == 1) {
            float2 e0 = endst[pl];
            float wr_ = e0.x, wi_ = e0.y;
#pragma unroll 8
            for (int t = 0; t < 64; t++) {
                float nr = lbr * wr_ - lbi * wi_, ni = lbr * wi_ + lbi * wr_;
                wr_ = nr; wi_ = ni;
                float2 v = bu[(64 + t) * 128 + pl];
                v.x += wr_; v.y += wi_;
                hloc[(size_t)(r0 + 64 + t) * P + p] = v;
                if (t == 63) Ec[rb * P + p] = v;
            }
        }
    }
}

__global__ void fix_k(float2* hloc, const float2* Ec, const u16* Lre, const u16* Lim, const u16* logdt) {
    int p = threadIdx.x + blockIdx.x * blockDim.x;
    float lbr, lbi; lam_bar(Lre, Lim, logdt, p, 1.0f, lbr, lbi);
    float l128r, l128i; lam_bar(Lre, Lim, logdt, p, 128.0f, l128r, l128i);
    float sr = 0, si = 0;
    for (int c = 0; c < 16; c++) {
        float wr_ = sr, wi_ = si;
        for (int t = 0; t < 128; t++) {
            float nr = lbr * wr_ - lbi * wi_, ni = lbr * wi_ + lbi * wr_; wr_ = nr; wi_ = ni;
            float2 v = hloc[(c * 128 + t) * P + p]; v.x += wr_; v.y += wi_; hloc[(c * 128 + t) * P + p] = v;
        }
        float2 e = Ec[c * P + p];
        float nr = l128r * sr - l128i * si + e.x, ni = l128r * si + l128i * sr + e.y; sr = nr; si = ni;
    }
}
__global__ void fin_k(const u16* x, const u16* xn, const float2* hs, const u16* h,
                      const u16* Cre, const u16* Cim, const u16* wd, const u16* Dsk, float* out) {
    int t = blockIdx.x; int d = threadIdx.x;
    float y = 0;
    for (int p = 0; p < P; p++) { float2 hv = hs[t * P + p]; y += hv.x * bf2f(Cre[d * P + p]) - hv.y * bf2f(Cim[d * P + p]); }
    float dn = 0;
    for (int f = 0; f < F; f++) dn += bf2f(h[t * F + f]) * bf2f(wd[f * D + d]);
    dn = rbf(dn);
    float g = rbf(dn / (1.0f + expf(-dn)));
    float s = 2.0f * y + rbf(bf2f(xn[t * D + d]) * bf2f(Dsk[d]));
    out[t * D + d] = bf2f(x[t * D + d]) + s * g;
}

extern "C" void kernel_launch(void* const* d_in, const int* in_sizes, int n_in,
                              void* d_out, int out_size, void* d_ws, size_t ws_size,
                              hipStream_t stream) {
    const u16* x = (const u16*)d_in[0];
    const u16* Lre = (const u16*)d_in[1];
    const u16* Lim = (const u16*)d_in[2];
    const u16* Bre = (const u16*)d_in[3];
    const u16* Bim = (const u16*)d_in[4];
    const u16* Cre = (const u16*)d_in[5];
    const u16* Cim = (const u16*)d_in[6];
    const u16* logdt = (const u16*)d_in[7];
    const u16* Dsk = (const u16*)d_in[8];
    const u16* wg = (const u16*)d_in[10];
    const u16* wu = (const u16*)d_in[11];
    const u16* wd = (const u16*)d_in[12];
    char* ws = (char*)d_ws;
    u16* xn = (u16*)ws;
    float2* hloc = (float2*)(ws + (4 << 20));
    u16* h = (u16*)(ws + (16 << 20));
    float2* Ec = (float2*)(ws + (24 << 20));
    k1<<<256, 256, 0, stream>>>(x, Lre, Lim, Bre, Bim, logdt, wg, wu, xn, h, hloc, Ec);
    fix_k<<<8, 64, 0, stream>>>(hloc, Ec, Lre, Lim, logdt);
    fin_k<<<L, D, 0, stream>>>(x, xn, hloc, h, Cre, Cim, wd, Dsk, (float*)d_out);
}
```

```cpp
#include <hip/hip_runtime.h>
#include <hip/hip_bf16.h>
#include <stdint.h>

#define L 2048
#define D 512
#define P 512
#define F 1792
typedef unsigned short u16;
typedef short s16x8 __attribute__((ext_vector_type(8)));
typedef short s16x4 __attribute__((ext_vector_type(4)));
typedef float f32x16 __attribute__((ext_vector_type(16)));
typedef unsigned u32x4 __attribute__((ext_vector_type(4)));
typedef __attribute__((address_space(3))) s16x4* lds_s16x4_p;

__device__ __forceinline__ float bf2f(u16 v) { return __uint_as_float(((unsigned)v) << 16); }
__device__ __forceinline__ u16 f2bf(float f) {
    unsigned u = __float_as_uint(f);
    u += 0x7fffu + ((u >> 16) & 1u);
    return (u16)(u >> 16);
}
__device__ __forceinline__ float rbf(float f) { return bf2f(f2bf(f)); }

struct LamInfo { float lbr, lbi, cr, ci; };
__device__ __forceinline__ void lam_bar(const u16* Lre, const u16* Lim, const u16* logdt, int p, float mult,
                                        float& pr, float& pi) {
    float dt = rbf(expf(bf2f(logdt[p])));
    float lr = bf2f(Lre[p]), li = bf2f(Lim[p]);
    float er = expf(mult * (dt * lr)); float s, c; sincosf(mult * (dt * li), &s, &c);
    pr = er * c; pi = er * s;
}

#define MFMA32(a, b, c) __builtin_amdgcn_mfma_f32_32x32x16_bf16(a, b, c, 0, 0, 0)

constexpr int A_BYTES = 128 * 1024;
constexpr int BROW = 704;
constexpr int GDEPTH = 6;
constexpr int SDEPTH = 3;

__global__ __launch_bounds__(256, 1) void k1(const u16* __restrict__ x, const u16* __restrict__ Lre, const u16* __restrict__ Lim,
        const u16* __restrict__ Bre, const u16* __restrict__ Bim, const u16* __restrict__ logdt,
        const u16* __restrict__ wg, const u16* __restrict__ wu,
        u16* __restrict__ xn_out, u16* __restrict__ h_out, float2* __restrict__ hloc, float2* __restrict__ Ec) {
    __shared__ __attribute__((aligned(16))) char smem[163840];
    const int tid = threadIdx.x, lane = tid & 63, wid = tid >> 6;
    const int b = blockIdx.x, idx = b >> 3;
    const int slot = 2 * (b & 7) + (idx & 1), rb = idx >> 1;
    const int r0 = rb * 128;
    const int wr = wid >> 1, wc = wid & 1;
    char* Bs = smem + A_BYTES;

    const bool is_ssm = slot < 4;
    u32x4 ring[GDEPTH][3];
    const u16* gsrc[4]; int ldst[4];
    int f0 = 0, p0 = 0;
    if (!is_ssm) {
        int g = slot - 4; f0 = min(g * 160, F - 160);
#pragma unroll
        for (int i = 0; i < 3; i++) {
            int c = tid + 256 * i; if (c >= 640) c = tid;
            int k = c / 40, cc = c % 40;
            int unit = cc >> 2, part = (cc >> 1) & 1, half = cc & 1;
            gsrc[i] = (part ? wu : wg) + k * F + f0 + 16 * unit + 8 * half;
            ldst[i] = k * BROW + cc * 16;
        }
#pragma unroll
        for (int s = 0; s < GDEPTH; s++)
#pragma unroll
            for (int i = 0; i < 3; i++) ring[s][i] = *(const u32x4*)(gsrc[i] + s * 16 * F);
    } else {
        p0 = slot * 128;
#pragma unroll
        for (int i = 0; i < 4; i++) {
            int c = tid + 256 * i; int n = c >> 2, ch = c & 3;
            gsrc[i] = (n < 128 ? Bre : Bim) + (p0 + (n & 127)) * D + ch * 8;
            ldst[i] = n * 64 + ((ch ^ ((n >> 2) & 3)) << 4);
        }
    }
    u32x4 sring[SDEPTH][4];
    if (is_ssm) {
#pragma unroll
        for (int s = 0; s < SDEPTH; s++)
#pragma unroll
            for (int i = 0; i < 4; i++) sring[s][i] = *(const u32x4*)(gsrc[i] + s * 32);
    }

#pragma unroll
    for (int half = 0; half < 2; half++) {
        u32x4 xv[16];
#pragma unroll
        for (int i = 0; i < 16; i++) xv[i] = *(const u32x4*)(x + (size_t)(r0 + wid * 32 + half * 16 + i) * D + lane * 8);
#pragma unroll
        for (int i = 0; i < 16; i++) {
            int row = wid * 32 + half * 16 + i;
            float v[8];
#pragma unroll
            for (int e = 0; e < 4; e++) { v[2 * e] = __uint_as_float(xv[i][e] << 16); v[2 * e + 1] = __uint_as_float(xv[i][e] & 0xffff0000u); }
            float s = 0;
#pragma unroll
            for (int e = 0; e < 8; e++) s += v[e];
#pragma unroll
            for (int o = 32; o > 0; o >>= 1) s += __shfl_xor(s, o);
            float mean = s * (1.0f / D);
            float q = 0;
#pragma unroll
            for (int e = 0; e < 8; e++) { float d = v[e] - mean; q += d * d; }
#pragma unroll
            for (int o = 32; o > 0; o >>= 1) q += __shfl_xor(q, o);
            float mb = rbf(mean), vb = rbf(q * (1.0f / D));
            float den = rbf(sqrtf(rbf(vb + 1e-5f)));
            u32x4 o4;
#pragma unroll
            for (int e = 0; e < 4; e++) {
                u16 lo = f2bf(rbf(v[2 * e] - mb) / den), hi = f2bf(rbf(v[2 * e + 1] - mb) / den);
                o4[e] = (unsigned)lo | ((unsigned)hi << 16);
            }
            *(u32x4*)(smem + row * 1024 + (((lane & 48) | ((lane ^ row) & 15)) << 4)) = o4;
            if (slot == 4) *(u32x4*)(xn_out + (size_t)(r0 + row) * D + lane * 8) = o4;
        }
    }

    const int l31 = lane & 31, lh = lane >> 5;
    if (!is_ssm) {
        f32x16 acc[2][5];
#pragma unroll
        for (int m = 0; m < 2; m++)
#pragma unroll
            for (int n = 0; n < 5; n++)
#pragma unroll
                for (int e = 0; e < 16; e++) acc[m][n][e] = 0.f;
#pragma unroll
        for (int i = 0; i < 3; i++) *(u32x4*)(Bs + ldst[i]) = ring[0][i];
#pragma unroll
        for (int i = 0; i < 3; i++) ring[0][i] = *(const u32x4*)(gsrc[i] + GDEPTH * 16 * F);
        __syncthreads();
        const unsigned trb = (unsigned)(size_t)Bs + (8 * lh + ((lane & 15) >> 2)) * BROW + (wc * 160 + 16 * ((lane >> 4) & 1) + 4 * (lane & 3)) * 2;
#pragma unroll
        for (int s = 0; s < 32; s++) {
            const int cur = s & 1;
            if (s + 1 < 32) {
#pragma unroll
                for (int i = 0; i < 3; i++) *(u32x4*)(Bs + (cur ^ 1) * (16 * BROW) + ldst[i]) = ring[(s + 1) % GDEPTH][i];
                if (s + 1 + GDEPTH < 32) {
#pragma unroll
                    for (int i = 0; i < 3; i++) ring[(s + 1) % GDEPTH][i] = *(const u32x4*)(gsrc[i] + (s + 1 + GDEPTH) * 16 * F);
                }
            }
            s16x8 af[2];
#pragma unroll
            for (int m = 0; m < 2; m++) {
                int row = wr * 64 + m * 32 + l31; int ch = 2 * s + lh;
                af[m] = *(const s16x8*)(smem + row * 1024 + (((ch & 48) | ((ch ^ row) & 15)) << 4));
            }
#pragma unroll
            for (int n = 0; n < 5; n++) {
                s16x4 b0 = __builtin_amdgcn_ds_read_tr16_b64_v4i16((lds_s16x4_p)(trb + cur * (16 * BROW) + n * 64));
                s16x4 b1 = __builtin_amdgcn_ds_read_tr16_b64_v4i16((lds_s16x4_p)(trb + cur * (16 * BROW) + n * 64 + 4 * BROW));
                s16x8 bf = __builtin_shufflevector(b0, b1, 0, 1, 2, 3, 4, 5, 6, 7);
#pragma unroll
                for (int m = 0; m < 2; m++) acc[m][n] = MFMA32(af[m], bf, acc[m][n]);
            }
            __syncthreads();
        }
#pragma unroll
        for (int m = 0; m < 2; m++)
#pragma unroll
            for (int n = 0; n < 5; n++)
#pragma unroll
                for (int e = 0; e < 16; e++) {
                    float mine = rbf(acc[m][n][e]);
                    float other = __shfl_xor(mine, 16);
                    if (!(lane & 16)) {
                        int row = wr * 64 + m * 32 + (e & 3) + 8 * (e >> 2) + 4 * lh;
                        h_out[(size_t)(r0 + row) * F + f0 + (wc * 5 + n) * 16 + (lane & 15)] = f2bf(mine * other);
                    }
                }
    } else {
        f32x16 acc[2][4];
#pragma unroll
        for (int m = 0; m < 2; m++)
#pragma unroll
            for (int n = 0; n < 4; n++)
#pragma unroll
                for (int e = 0; e < 16; e++) acc[m][n][e] = 0.f;
#pragma unroll
        for (int i = 0; i < 4; i++) *(u32x4*)(Bs + ldst[i]) = sring[0][i];
#pragma unroll
        for (int i = 0; i < 4; i++) sring[0][i] = *(const u32x4*)(gsrc[i] + SDEPTH * 32);
        __syncthreads();
#pragma unroll
        for (int s = 0; s < 16; s++) {
            const int cur = s & 1;
            if (s + 1 < 16) {
#pragma unroll
                for (int i = 0; i < 4; i++) *(u32x4*)(Bs + (cur ^ 1) * 16384 + ldst[i]) = sring[(s + 1) % SDEPTH][i];
                if (s + 1 + SDEPTH < 16) {
#pragma unroll
                    for (int i = 0; i < 4; i++) sring[(s + 1) % SDEPTH][i] = *(const u32x4*)(gsrc[i] + (s + 1 + SDEPTH) * 32);
                }
            }
#pragma unroll
            for (int kk = 0; kk < 2; kk++) {
                s16x8 af[2];
#pragma unroll
                for (int m = 0; m < 2; m++) {
                    int row = wr * 64 + m * 32 + l31; int ch = 4 * s + 2 * kk + lh;
                    af[m] = *(const s16x8*)(smem + row * 1024 + (((ch & 48) | ((ch ^ row) & 15)) << 4));
                }
#pragma unroll
                for (int n = 0; n < 4; n++) {
                    int nn = (n >> 1) * 128 + wc * 64 + (n & 1) * 32 + l31; int ch = 2 * kk + lh;
                    s16x8 bf = *(const s16x8*)(Bs + cur * 16384 + nn * 64 + ((ch ^ ((nn >> 2) & 3)) << 4));
#pragma unroll
                    for (int m = 0; m < 2; m++) acc[m][n] = MFMA32(af[m], bf, acc[m][n]);
                }
            }
            __syncthreads();
        }
        float2* bu = (float2*)smem;
#pragma unroll
        for (int j = 0; j < 2; j++) {
            int pl = wc * 64 + j * 32 + l31; int p = p0 + pl;
            float dt = rbf(expf(bf2f(logdt[p])));
            float lr = bf2f(Lre[p]), li = bf2f(Lim[p]);
            float er = expf(dt * lr); float sn, cs; sincosf(dt * li, &sn, &cs);
            float br = er * cs - 1.0f, bi = er * sn;
            float dn = lr * lr + li * li;
            float cr = (br * lr + bi * li) / dn, ci = (bi * lr - br * li) / dn;
#pragma unroll
            for (int m = 0; m < 2; m++)
#pragma unroll
                for (int e = 0; e < 16; e++) {
                    int row = wr * 64 + m * 32 + (e & 3) + 8 * (e >> 2) + 4 * lh;
                    float ar = acc[m][j][e], ai = acc[m][2 + j][e];
                    bu[row * 128 + pl] = make_float2(cr * ar - ci * ai, cr * ai + ci * ar);
                }
        }
        __syncthreads();
        const int pl = tid & 127, hf = tid >> 7, p = p0 + pl;
        float lbr, lbi; lam_bar(Lre, Lim, logdt, p, 1.0f, lbr, lbi);
        float hr = 0, hi = 0;
#pragma unroll 8
        for (int t = 0; t < 64; t++) {
            float2 v = bu[(hf * 64 + t) * 128 + pl];
            float nr = lbr * hr - lbi * hi + v.x, ni = lbr * hi + lbi * hr + v.y;
            hr = nr; hi = ni;
            if (hf == 0) hloc[(size_t)(r0 + t) * P + p] = make_float2(hr, hi);
            else bu[(64 + t) * 128 + pl] = make_float2(hr, hi);
        }
        float2* endst = (float2*)Bs;
        if (hf == 0) endst[pl] = make_float2(hr, hi);
        __syncthreads();
        if (hf == 1) {
            float2 e0 = endst[pl];
            float wr_ = e0.x, wi_ = e0.y;
#pragma unroll 8
            for (int t = 0; t < 64; t++) {
                float nr = lbr * wr_ - lbi * wi_, ni = lbr * wi_ + lbi * wr_;
                wr_ = nr; wi_ = ni;
                float2 v = bu[(64 + t) * 128 + pl];
                v.x += wr_; v.y += wi_;
                hloc[(size_t)(r0 + 64 + t) * P + p] = v;
                if (t == 63) Ec[rb * P + p] = v;
            }
        }
    }
}


constexpr int K2D = 6;
__global__ __launch_bounds__(256, 1) void k2(const u16* __restrict__ x, const u16* __restrict__ xn, const u16* __restrict__ h,
        const float2* __restrict__ hloc, const float2* __restrict__ Ec, const u16* __restrict__ Lre, const u16* __restrict__ Lim,
        const u16* __restrict__ logdt, const u16* __restrict__ Cre, const u16* __restrict__ Cim, const u16* __restrict__ wd,
        const u16* __restrict__ Dsk, float* __restrict__ out) {
    __shared__ __attribute__((aligned(16))) char smem[2 * 16384 + 4096];
    float2* Sc = (float2*)(smem + 32768);
    const int tid = threadIdx.x, lane = tid & 63, wid = tid >> 6;
    const int b = blockIdx.x, idx = b >> 3;
    const int tb = 4 * (b & 7) + (idx >> 3), db = idx & 7;
    const int t0 = tb * 64, d0 = db * 64;
    const int wr = wid >> 1, wc = wid & 1, l31 = lane & 31, lh = lane >> 5;
    const u16* asrc[2]; const u16* bsrc[2]; int adst[2];
#pragma unroll
    for (int i = 0; i < 2; i++) {
        int c = tid + 256 * i, r = c >> 3, ch = c & 7;
        asrc[i] = h + (size_t)(t0 + r) * F + ch * 8;
        bsrc[i] = wd + (size_t)r * D + d0 + ch * 8;
        adst[i] = r * 128 + ((ch ^ (r & 7)) << 4);
    }
    u32x4 ring[K2D][4];
#pragma unroll
    for (int s = 0; s < K2D; s++)
#pragma unroll
        for (int i = 0; i < 2; i++) { ring[s][i] = *(const u32x4*)(asrc[i] + s * 64); ring[s][2 + i] = *(const u32x4*)(bsrc[i] + (size_t)s * 64 * D); }
    const int ck = t0 >> 7, off = t0 & 127;
    for (int p = tid; p < P; p += 256) {
        float l128r, l128i; lam_bar(Lre, Lim, logdt, p, 128.0f, l128r, l128i);
        float sr = 0, si = 0;
        for (int c = 0; c < ck; c++) {
            float2 e = Ec[c * P + p];
            float nr = l128r * sr - l128i * si + e.x, ni = l128r * si + l128i * sr + e.y; sr = nr; si = ni;
        }
        Sc[p] = make_float2(sr, si);
    }
    f32x16 acc;
#pragma unroll
    for (int e = 0; e < 16; e++) acc[e] = 0.f;
#pragma unroll
    for (int i = 0; i < 2; i++) { *(u32x4*)(smem + adst[i]) = ring[0][i]; *(u32x4*)(smem + 8192 + (tid + 256 * i) * 16) = ring[0][2 + i]; }
#pragma unroll
    for (int i = 0; i < 2; i++) { ring[0][i] = *(const u32x4*)(asrc[i] + K2D * 64); ring[0][2 + i] = *(const u32x4*)(bsrc[i] + (size_t)K2D * 64 * D); }
    __syncthreads();
    const unsigned trb = (unsigned)(size_t)smem + 8192 + (8 * lh + ((lane & 15) >> 2)) * 128 + (wc * 32 + 16 * ((lane >> 4) & 1) + 4 * (lane & 3)) * 2;
    const int arow = wr * 32 + l31;
    const int sp = tid & 31, strun = (tid >> 5) * 8;
#pragma unroll
    for (int s = 0; s < 28; s++) {
        const int cur = s & 1;
        if (s + 1 < 28) {
#pragma unroll
            for (int i = 0; i < 2; i++) { *(u32x4*)(smem + (cur ^ 1) * 16384 + adst[i]) = ring[(s + 1) % K2D][i]; *(u32x4*)(smem + (cur ^ 1) * 16384 + 8192 + (tid + 256 * i) * 16) = ring[(s + 1) % K2D][2 + i]; }
            if (s + 1 + K2D < 28) {
#pragma unroll
                for (int i = 0; i < 2; i++) { ring[(s + 1) % K2D][i] = *(const u32x4*)(asrc[i] + (s + 1 + K2D) * 64); ring[(s + 1) % K2D][2 + i] = *(const u32x4*)(bsrc[i] + (size_t)(s + 1 + K2D) * 64 * D); }
            }
        }
#pragma unroll
        for (int kk = 0; kk < 4; kk++) {
            int ch = 2 * kk + lh;
            s16x8 af = *(const s16x8*)(smem + cur * 16384 + arow * 128 + ((ch ^ (arow & 7)) << 4));
            s16x4 b0 = __builtin_amdgcn_ds_read_tr16_b64_v4i16((lds_s16x4_p)(trb + cur * 16384 + kk * 16 * 128));
            s16x4 b1 = __builtin_amdgcn_ds_read_tr16_b64_v4i16((lds_s16x4_p)(trb + cur * 16384 + kk * 16 * 128 + 4 * 128));
            s16x8 bf = __builtin_shufflevector(b0, b1, 0, 1, 2, 3, 4, 5, 6, 7);
            acc = MFMA32(af, bf, acc);
        }
        __syncthreads();
    }
    f32x16 accy;
#pragma unroll
    for (int e = 0; e < 16; e++) accy[e] = 0.f;
    float2 hv[2][8]; u32x4 cv[2][2];
    const u16* csrc[2]; int cdst[2];
#pragma unroll
    for (int i = 0; i < 2; i++) {
        int c = tid + 256 * i, r = c >> 3, ch = c & 7;
        csrc[i] = (ch < 4 ? Cre : Cim) + (size_t)(d0 + r) * P + (ch & 3) * 8;
        cdst[i] = 8192 + r * 128 + ((ch ^ (r & 7)) << 4);
    }
#pragma unroll
    for (int j = 0; j < 8; j++) hv[0][j] = hloc[(size_t)(t0 + strun + j) * P + sp];
#pragma unroll
    for (int i = 0; i < 2; i++) cv[0][i] = *(const u32x4*)(csrc[i]);
#pragma unroll
    for (int s = 0; s < 16; s++) {
        const int cur = s & 1;
        if (s + 1 < 16) {
#pragma unroll
            for (int j = 0; j < 8; j++) hv[cur ^ 1][j] = hloc[(size_t)(t0 + strun + j) * P + (s + 1) * 32 + sp];
#pragma unroll
            for (int i = 0; i < 2; i++) cv[cur ^ 1][i] = *(const u32x4*)(csrc[i] + (s + 1) * 32);
        }
        {
            int p = s * 32 + sp;
            float lbr, lbi; lam_bar(Lre, Lim, logdt, p, 1.0f, lbr, lbi);
            float pr, pi; lam_bar(Lre, Lim, logdt, p, (float)(off + strun), pr, pi);
            float2 S = Sc[p];
            float wr_ = pr * S.x - pi * S.y, wi_ = pr * S.y + pi * S.x;
#pragma unroll
            for (int j = 0; j < 8; j++) {
                float nr = lbr * wr_ - lbi * wi_, ni = lbr * wi_ + lbi * wr_; wr_ = nr; wi_ = ni;
                int r = strun + j;
                char* rowp = smem + cur * 16384 + r * 128;
                *(u16*)(rowp + ((((sp >> 3)) ^ (r & 7)) << 4) + (sp & 7) * 2) = f2bf(hv[cur][j].x + wr_);
                *(u16*)(rowp + (((4 + (sp >> 3)) ^ (r & 7)) << 4) + (sp & 7) * 2) = f2bf(-(hv[cur][j].y + wi_));
            }
#pragma unroll
            for (int i = 0; i < 2; i++) *(u32x4*)(smem + cur * 16384 + cdst[i]) = cv[cur][i];
        }
        __syncthreads();
#pragma unroll
        for (int kk = 0; kk < 4; kk++) {
            int ch = 2 * kk + lh; int brow = wc * 32 + l31;
            s16x8 af = *(const s16x8*)(smem + cur * 16384 + arow * 128 + ((ch ^ (arow & 7)) << 4));
            s16x8 bf = *(const s16x8*)(smem + cur * 16384 + 8192 + brow * 128 + ((ch ^ (brow & 7)) << 4));
            accy = MFMA32(af, bf, accy);
        }
    }
    const int d = d0 + wc * 32 + l31;
    const float dsk = bf2f(Dsk[d]);
#pragma unroll
    for (int e = 0; e < 16; e++) {
        int t = t0 + wr * 32 + (e & 3) + 8 * (e >> 2) + 4 * lh;
        float dn = rbf(acc[e]);
        float g = rbf(dn / (1.0f + expf(-dn)));
        float sv = 2.0f * accy[e] + rbf(bf2f(xn[(size_t)t * D + d]) * dsk);
        out[(size_t)t * D + d] = bf2f(x[(size_t)t * D + d]) + sv * g;
    }
}

extern "C" void kernel_launch(void* const* d_in, const int* in_sizes, int n_in,
                              void* d_out, int out_size, void* d_ws, size_t ws_size,
                              hipStream_t stream) {
    const u16* x = (const u16*)d_in[0];
    const u16* Lre = (const u16*)d_in[1];
    const u16* Lim = (const u16*)d_in[2];
    const u16* Bre = (const u16*)d_in[3];
    const u16* Bim = (const u16*)d_in[4];
    const u16* Cre = (const u16*)d_in[5];
    const u16* Cim = (const u16*)d_in[6];
    const u16* logdt = (const u16*)d_in[7];
    const u16* Dsk = (const u16*)d_in[8];
    const u16* wg = (const u16*)d_in[10];
    const u16* wu = (const u16*)d_in[11];
    const u16* wd = (const u16*)d_in[12];
    char* ws = (char*)d_ws;
    u16* xn = (u16*)ws;
    float2* hloc = (float2*)(ws + (4 << 20));
    u16* h = (u16*)(ws + (16 << 20));
    float2* Ec = (float2*)(ws + (24 << 20));
    k1<<<256, 256, 0, stream>>>(x, Lre, Lim, Bre, Bim, logdt, wg, wu, xn, h, hloc, Ec);
    k2<<<256, 256, 0, stream>>>(x, xn, h, hloc, Ec, Lre, Lim, logdt, Cre, Cim, wd, Dsk, (float*)d_out);
}
```

```cpp
#include <hip/hip_runtime.h>
#include <hip/hip_bf16.h>
#include <stdint.h>

#define L 2048
#define D 512
#define P 512
#define F 1792
typedef unsigned short u16;
typedef short s16x8 __attribute__((ext_vector_type(8)));
typedef short s16x4 __attribute__((ext_vector_type(4)));
typedef float f32x16 __attribute__((ext_vector_type(16)));
typedef unsigned u32x4 __attribute__((ext_vector_type(4)));
typedef __attribute__((address_space(3))) s16x4* lds_s16x4_p;

__device__ __forceinline__ float bf2f(u16 v) { return __uint_as_float(((unsigned)v) << 16); }
__device__ __forceinline__ u16 f2bf(float f) { return __builtin_bit_cast(u16, (__bf16)f); }
__device__ __forceinline__ float rbf(float f) { return bf2f(f2bf(f)); }

struct LamInfo { float lbr, lbi, cr, ci; };
__device__ __forceinline__ void lam_bar(const u16* Lre, const u16* Lim, const u16* logdt, int p, float mult,
                                        float& pr, float& pi) {
    float dt = rbf(expf(bf2f(logdt[p])));
    float lr = bf2f(Lre[p]), li = bf2f(Lim[p]);
    float er = expf(mult * (dt * lr)); float s, c; sincosf(mult * (dt * li), &s, &c);
    pr = er * c; pi = er * s;
}

#define MFMA32(a, b, c) __builtin_amdgcn_mfma_f32_32x32x16_bf16(a, b, c, 0, 0, 0)


__global__ __launch_bounds__(256) void ln_k2(const u16* __restrict__ x, u16* __restrict__ xn) {
    const int t = blockIdx.x * 4 + (threadIdx.x >> 6), lane = threadIdx.x & 63;
    u32x4 xv = *(const u32x4*)(x + (size_t)t * D + lane * 8);
    float v[8];
#pragma unroll
    for (int e = 0; e < 4; e++) { v[2 * e] = __uint_as_float(xv[e] << 16); v[2 * e + 1] = __uint_as_float(xv[e] & 0xffff0000u); }
    float s = 0;
#pragma unroll
    for (int e = 0; e < 8; e++) s += v[e];
#pragma unroll
    for (int o = 32; o > 0; o >>= 1) s += __shfl_xor(s, o);
    float mean = s * (1.0f / D), q = 0;
#pragma unroll
    for (int e = 0; e < 8; e++) { float d = v[e] - mean; q += d * d; }
#pragma unroll
    for (int o = 32; o > 0; o >>= 1) q += __shfl_xor(q, o);
    float mb = rbf(mean), vb = rbf(q * (1.0f / D));
    float den = rbf(sqrtf(rbf(vb + 1e-5f)));
    u32x4 o4;
#pragma unroll
    for (int e = 0; e < 4; e++) o4[e] = (unsigned)f2bf(rbf(v[2 * e] - mb) / den) | ((unsigned)f2bf(rbf(v[2 * e + 1] - mb) / den) << 16);
    *(u32x4*)(xn + (size_t)t * D + lane * 8) = o4;
}
constexpr int A_BYTES = 128 * 1024;
constexpr int BROW = 704;
constexpr int GDEPTH = 6;
constexpr int SDEPTH = 3;

__global__ __launch_bounds__(256, 1) void k1(const u16* __restrict__ x, const u16* __restrict__ Lre, const u16* __restrict__ Lim,
        const u16* __restrict__ Bre, const u16* __restrict__ Bim, const u16* __restrict__ logdt,
        const u16* __restrict__ wg, const u16* __restrict__ wu,
        u16* __restrict__ xn_out, u16* __restrict__ h_out, float2* __restrict__ hloc, float2* __restrict__ Ec) {
    __shared__ __attribute__((aligned(16))) char smem[163840];
    const int tid = threadIdx.x, lane = tid & 63, wid = tid >> 6;
    const int b = blockIdx.x, idx = b >> 3;
    const int slot = 2 * (b & 7) + (idx & 1), rb = idx >> 1;
    const int r0 = rb * 128;
    const int wr = wid >> 1, wc = wid & 1;
    char* Bs = smem + A_BYTES;

    const bool is_ssm = slot < 4;
    u32x4 ring[GDEPTH][3];
    const u16* gsrc[4]; int ldst[4];
    int f0 = 0, p0 = 0;
    if (!is_ssm) {
        int g = slot - 4; f0 = min(g * 160, F - 160);
#pragma unroll
        for (int i = 0; i < 3; i++) {
            int c = tid + 256 * i; if (c >= 640) c = tid;
            int k = c / 40, cc = c % 40;
            int unit = cc >> 2, part = (cc >> 1) & 1, half = cc & 1;
            gsrc[i] = (part ? wu : wg) + k * F + f0 + 16 * unit + 8 * half;
            ldst[i] = k * BROW + cc * 16;
        }
#pragma unroll
        for (int s = 0; s < GDEPTH; s++)
#pragma unroll
            for (int i = 0; i < 3; i++) ring[s][i] = *(const u32x4*)(gsrc[i] + s * 16 * F);
    } else {
        p0 = slot * 128;
#pragma unroll
        for (int i = 0; i < 4; i++) {
            int c = tid + 256 * i; int n = c >> 2, ch = c & 3;
            gsrc[i] = (n < 128 ? Bre : Bim) + (p0 + (n & 127)) * D + ch * 8;
            ldst[i] = n * 64 + ((ch ^ ((n >> 2) & 3)) << 4);
        }
    }
    u32x4 sring[SDEPTH][4];
    if (is_ssm) {
#pragma unroll
        for (int s = 0; s < SDEPTH; s++)
#pragma unroll
            for (int i = 0; i < 4; i++) sring[s][i] = *(const u32x4*)(gsrc[i] + s * 32);
    }

#pragma unroll 8
    for (int i = 0; i < 32; i++) {
        int row = i * 4 + wid;
        u32x4 v = *(const u32x4*)(xn_out + (size_t)(r0 + row) * D + lane * 8);
        *(u32x4*)(smem + row * 1024 + (((lane & 48) | ((lane ^ row) & 15)) << 4)) = v;
    }

    const int l31 = lane & 31, lh = lane >> 5;
    if (!is_ssm) {
        f32x16 acc[2][5];
#pragma unroll
        for (int m = 0; m < 2; m++)
#pragma unroll
            for (int n = 0; n < 5; n++)
#pragma unroll
                for (int e = 0; e < 16; e++) acc[m][n][e] = 0.f;
#pragma unroll
        for (int i = 0; i < 3; i++) *(u32x4*)(Bs + ldst[i]) = ring[0][i];
#pragma unroll
        for (int i = 0; i < 3; i++) ring[0][i] = *(const u32x4*)(gsrc[i] + GDEPTH * 16 * F);
        __syncthreads();
        const unsigned trb = (unsigned)(size_t)Bs + (8 * lh + ((lane & 15) >> 2)) * BROW + (wc * 160 + 16 * ((lane >> 4) & 1) + 4 * (lane & 3)) * 2;
#pragma unroll
        for (int s = 0; s < 32; s++) {
            const int cur = s & 1;
            if (s + 1 < 32) {
#pragma unroll
                for (int i = 0; i < 3; i++) *(u32x4*)(Bs + (cur ^ 1) * (16 * BROW) + ldst[i]) = ring[(s + 1) % GDEPTH][i];
                if (s + 1 + GDEPTH < 32) {
#pragma unroll
                    for (int i = 0; i < 3; i++) ring[(s + 1) % GDEPTH][i] = *(const u32x4*)(gsrc[i] + (s + 1 + GDEPTH) * 16 * F);
                }
            }
            s16x8 af[2];
#pragma unroll
            for (int m = 0; m < 2; m++) {
                int row = wr * 64 + m * 32 + l31; int ch = 2 * s + lh;
                af[m] = *(const s16x8*)(smem + row * 1024 + (((ch & 48) | ((ch ^ row) & 15)) << 4));
            }
#pragma unroll
            for (int n = 0; n < 5; n++) {
                s16x4 b0 = __builtin_amdgcn_ds_read_tr16_b64_v4i16((lds_s16x4_p)(trb + cur * (16 * BROW) + n * 64));
                s16x4 b1 = __builtin_amdgcn_ds_read_tr16_b64_v4i16((lds_s16x4_p)(trb + cur * (16 * BROW) + n * 64 + 4 * BROW));
                s16x8 bf = __builtin_shufflevector(b0, b1, 0, 1, 2, 3, 4, 5, 6, 7);
#pragma unroll
                for (int m = 0; m < 2; m++) acc[m][n] = MFMA32(af[m], bf, acc[m][n]);
            }
            __syncthreads();
        }
        u16* ht = (u16*)smem;
#pragma unroll
        for (int m = 0; m < 2; m++)
#pragma unroll
            for (int n = 0; n < 5; n++)
#pragma unroll
                for (int e = 0; e < 16; e++) {
                    int row = wr * 64 + m * 32 + (e & 3) + 8 * (e >> 2) + 4 * lh;
                    ht[row * 328 + (wc * 5 + n) * 32 + l31] = f2bf(acc[m][n][e]);
                }
        __syncthreads();
        for (int c = tid; c < 128 * 20; c += 256) {
            int row = c / 20, ch = c % 20, u = ch >> 1, hh = ch & 1;
            u32x4 g4 = *(const u32x4*)(ht + row * 328 + u * 32 + hh * 8), u4 = *(const u32x4*)(ht + row * 328 + u * 32 + 16 + hh * 8), o4;
#pragma unroll
            for (int e = 0; e < 4; e++) {
                u16 lo = f2bf(__uint_as_float(g4[e] << 16) * __uint_as_float(u4[e] << 16));
                u16 hi = f2bf(__uint_as_float(g4[e] & 0xffff0000u) * __uint_as_float(u4[e] & 0xffff0000u));
                o4[e] = (unsigned)lo | ((unsigned)hi << 16);
            }
            *(u32x4*)(h_out + (size_t)(r0 + row) * F + f0 + ch * 8) = o4;
        }
    } else {
        f32x16 acc[2][4];
#pragma unroll
        for (int m = 0; m < 2; m++)
#pragma unroll
            for (int n = 0; n < 4; n++)
#pragma unroll
                for (int e = 0; e < 16; e++) acc[m][n][e] = 0.f;
#pragma unroll
        for (int i = 0; i < 4; i++) *(u32x4*)(Bs + ldst[i]) = sring[0][i];
#pragma unroll
        for (int i = 0; i < 4; i++) sring[0][i] = *(const u32x4*)(gsrc[i] + SDEPTH * 32);
        __syncthreads();
#pragma unroll
        for (int s = 0; s < 16; s++) {
            const int cur = s & 1;
            if (s + 1 < 16) {
#pragma unroll
                for (int i = 0; i < 4; i++) *(u32x4*)(Bs + (cur ^ 1) * 16384 + ldst[i]) = sring[(s + 1) % SDEPTH][i];
                if (s + 1 + SDEPTH < 16) {
#pragma unroll
                    for (int i = 0; i < 4; i++) sring[(s + 1) % SDEPTH][i] = *(const u32x4*)(gsrc[i] + (s + 1 + SDEPTH) * 32);
                }
            }
#pragma unroll
            for (int kk = 0; kk < 2; kk++) {
                s16x8 af[2];
#pragma unroll
                for (int m = 0; m < 2; m++) {
                    int row = wr * 64 + m * 32 + l31; int ch = 4 * s + 2 * kk + lh;
                    af[m] = *(const s16x8*)(smem + row * 1024 + (((ch & 48) | ((ch ^ row) & 15)) << 4));
                }
#pragma unroll
                for (int n = 0; n < 4; n++) {
                    int nn = (n >> 1) * 128 + wc * 64 + (n & 1) * 32 + l31; int ch = 2 * kk + lh;
                    s16x8 bf = *(const s16x8*)(Bs + cur * 16384 + nn * 64 + ((ch ^ ((nn >> 2) & 3)) << 4));
#pragma unroll
                    for (int m = 0; m < 2; m++) acc[m][n] = MFMA32(af[m], bf, acc[m][n]);
                }
            }
            __syncthreads();
        }
        float2* bu = (float2*)smem;
#pragma unroll
        for (int j = 0; j < 2; j++) {
            int pl = wc * 64 + j * 32 + l31; int p = p0 + pl;
            float dt = rbf(expf(bf2f(logdt[p])));
            float lr = bf2f(Lre[p]), li = bf2f(Lim[p]);
            float er = expf(dt * lr); float sn, cs; sincosf(dt * li, &sn, &cs);
            float br = er * cs - 1.0f, bi = er * sn;
            float dn = lr * lr + li * li;
            float cr = (br * lr + bi * li) / dn, ci = (bi * lr - br * li) / dn;
#pragma unroll
            for (int m = 0; m < 2; m++)
#pragma unroll
                for (int e = 0; e < 16; e++) {
                    int row = wr * 64 + m * 32 + (e & 3) + 8 * (e >> 2) + 4 * lh;
                    float ar = acc[m][j][e], ai = acc[m][2 + j][e];
                    bu[row * 128 + pl] = make_float2(cr * ar - ci * ai, cr * ai + ci * ar);
                }
        }
        __syncthreads();
        const int pl = tid & 127, hf = tid >> 7, p = p0 + pl;
        float lbr, lbi; lam_bar(Lre, Lim, logdt, p, 1.0f, lbr, lbi);
        float hr = 0, hi = 0;
#pragma unroll 8
        for (int t = 0; t < 64; t++) {
            float2 v = bu[(hf * 64 + t) * 128 + pl];
            float nr = lbr * hr - lbi * hi + v.x, ni = lbr * hi + lbi * hr + v.y;
            hr = nr; hi = ni;
            if (hf == 0) hloc[(size_t)(r0 + t) * P + p] = make_float2(hr, hi);
            else bu[(64 + t) * 128 + pl] = make_float2(hr, hi);
        }
        float2* endst = (float2*)Bs;
        if (hf == 0) endst[pl] = make_float2(hr, hi);
        __syncthreads();
        if (hf == 1) {
            float2 e0 = endst[pl];
            float wr_ = e0.x, wi_ = e0.y;
#pragma unroll 8
            for (int t = 0; t < 64; t++) {
                float nr = lbr * wr_ - lbi * wi_, ni = lbr * wi_ + lbi * wr_;
                wr_ = nr; wi_ = ni;
                float2 v = bu[(64 + t) * 128 + pl];
                v.x += wr_; v.y += wi_;
                hloc[(size_t)(r0 + 64 + t) * P + p] = v;
                if (t == 63) Ec[rb * P + p] = v;
            }
        }
    }
}


constexpr int K2D = 6;
__global__ __launch_bounds__(256, 1) void k2(const u16* __restrict__ x, const u16* __restrict__ xn, const u16* __restrict__ h,
        const float2* __restrict__ hloc, const float2* __restrict__ Ec, const u16* __restrict__ Lre, const u16* __restrict__ Lim,
        const u16* __restrict__ logdt, const u16* __restrict__ Cre, const u16* __restrict__ Cim, const u16* __restrict__ wd,
        const u16* __restrict__ Dsk, float* __restrict__ out) {
    __shared__ __attribute__((aligned(16))) char smem[2 * 16384 + 32768 + 4096];
    float2* Wt = (float2*)(smem + 32768); float2* Lb = (float2*)(smem + 65536);
    const int tid = threadIdx.x, lane = tid & 63, wid = tid >> 6;
    const int b = blockIdx.x, idx = b >> 3;
    const int tb = 4 * (b & 7) + (idx >> 3), db = idx & 7;
    const int t0 = tb * 64, d0 = db * 64;
    const int wr = wid >> 1, wc = wid & 1, l31 = lane & 31, lh = lane >> 5;
    const u16* asrc[2]; const u16* bsrc[2]; int adst[2];
#pragma unroll
    for (int i = 0; i < 2; i++) {
        int c = tid + 256 * i, r = c >> 3, ch = c & 7;
        asrc[i] = h + (size_t)(t0 + r) * F + ch * 8;
        bsrc[i] = wd + (size_t)r * D + d0 + ch * 8;
        adst[i] = r * 128 + ((ch ^ (r & 7)) << 4);
    }
    u32x4 ring[K2D][4];
#pragma unroll
    for (int s = 0; s < K2D; s++)
#pragma unroll
        for (int i = 0; i < 2; i++) { ring[s][i] = *(const u32x4*)(asrc[i] + s * 64); ring[s][2 + i] = *(const u32x4*)(bsrc[i] + (size_t)s * 64 * D); }
    const int ck = t0 >> 7, off = t0 & 127;
    for (int p = tid; p < P; p += 256) {
        float2 ev[15];
#pragma unroll
        for (int c = 0; c < 15; c++) ev[c] = (c < ck) ? Ec[c * P + p] : make_float2(0.f, 0.f);
        float l128r, l128i; lam_bar(Lre, Lim, logdt, p, 128.0f, l128r, l128i);
        float sr = 0, si = 0;
#pragma unroll
        for (int c = 0; c < 15; c++) if (c < ck) { float nr = l128r * sr - l128i * si + ev[c].x, ni = l128r * si + l128i * sr + ev[c].y; sr = nr; si = ni; }
        float lbr, lbi; lam_bar(Lre, Lim, logdt, p, 1.0f, lbr, lbi); Lb[p] = make_float2(lbr, lbi);
        float pr, pi; lam_bar(Lre, Lim, logdt, p, (float)off, pr, pi);
        float l8r, l8i; lam_bar(Lre, Lim, logdt, p, 8.0f, l8r, l8i);
        float wr_ = pr * sr - pi * si, wi_ = pr * si + pi * sr;
#pragma unroll
        for (int r = 0; r < 8; r++) { Wt[r * P + p] = make_float2(wr_, wi_); float nr = l8r * wr_ - l8i * wi_, ni = l8r * wi_ + l8i * wr_; wr_ = nr; wi_ = ni; }
    }
    f32x16 acc;
#pragma unroll
    for (int e = 0; e < 16; e++) acc[e] = 0.f;
#pragma unroll
    for (int i = 0; i < 2; i++) { *(u32x4*)(smem + adst[i]) = ring[0][i]; *(u32x4*)(smem + 8192 + (tid + 256 * i) * 16) = ring[0][2 + i]; }
#pragma unroll
    for (int i = 0; i < 2; i++) { ring[0][i] = *(const u32x4*)(asrc[i] + K2D * 64); ring[0][2 + i] = *(const u32x4*)(bsrc[i] + (size_t)K2D * 64 * D); }
    __syncthreads();
    const unsigned trb = (unsigned)(size_t)smem + 8192 + (8 * lh + ((lane & 15) >> 2)) * 128 + (wc * 32 + 16 * ((lane >> 4) & 1) + 4 * (lane & 3)) * 2;
    const int arow = wr * 32 + l31;
    const int sp = tid & 31, strun = (tid >> 5) * 8;
#pragma unroll
    for (int s = 0; s < 28; s++) {
        const int cur = s & 1;
        if (s + 1 < 28) {
#pragma unroll
            for (int i = 0; i < 2; i++) { *(u32x4*)(smem + (cur ^ 1) * 16384 + adst[i]) = ring[(s + 1) % K2D][i]; *(u32x4*)(smem + (cur ^ 1) * 16384 + 8192 + (tid + 256 * i) * 16) = ring[(s + 1) % K2D][2 + i]; }
            if (s + 1 + K2D < 28) {
#pragma unroll
                for (int i = 0; i < 2; i++) { ring[(s + 1) % K2D][i] = *(const u32x4*)(asrc[i] + (s + 1 + K2D) * 64); ring[(s + 1) % K2D][2 + i] = *(const u32x4*)(bsrc[i] + (size_t)(s + 1 + K2D) * 64 * D); }
            }
        }
#pragma unroll
        for (int kk = 0; kk < 4; kk++) {
            int ch = 2 * kk + lh;
            s16x8 af = *(const s16x8*)(smem + cur * 16384 + arow * 128 + ((ch ^ (arow & 7)) << 4));
            s16x4 b0 = __builtin_amdgcn_ds_read_tr16_b64_v4i16((lds_s16x4_p)(trb + cur * 16384 + kk * 16 * 128));
            s16x4 b1 = __builtin_amdgcn_ds_read_tr16_b64_v4i16((lds_s16x4_p)(trb + cur * 16384 + kk * 16 * 128 + 4 * 128));
            s16x8 bf = __builtin_shufflevector(b0, b1, 0, 1, 2, 3, 4, 5, 6, 7);
            acc = MFMA32(af, bf, acc);
        }
        __syncthreads();
    }
    f32x16 accy;
#pragma unroll
    for (int e = 0; e < 16; e++) accy[e] = 0.f;
    float2 hv[2][8]; u32x4 cv[2][2];
    const u16* csrc[2]; int cdst[2];
#pragma unroll
    for (int i = 0; i < 2; i++) {
        int c = tid + 256 * i, r = c >> 3, ch = c & 7;
        csrc[i] = (ch < 4 ? Cre : Cim) + (size_t)(d0 + r) * P + (ch & 3) * 8;
        cdst[i] = 8192 + r * 128 + ((ch ^ (r & 7)) << 4);
    }
#pragma unroll
    for (int j = 0; j < 8; j++) hv[0][j] = hloc[(size_t)(t0 + strun + j) * P + sp];
#pragma unroll
    for (int i = 0; i < 2; i++) cv[0][i] = *(const u32x4*)(csrc[i]);
#pragma unroll
    for (int s = 0; s < 16; s++) {
        const int cur = s & 1;
        if (s + 1 < 16) {
#pragma unroll
            for (int j = 0; j < 8; j++) hv[cur ^ 1][j] = hloc[(size_t)(t0 + strun + j) * P + (s + 1) * 32 + sp];
#pragma unroll
            for (int i = 0; i < 2; i++) cv[cur ^ 1][i] = *(const u32x4*)(csrc[i] + (s + 1) * 32);
        }
        {
            int p = s * 32 + sp;
            float2 lb = Lb[p]; float lbr = lb.x, lbi = lb.y; float2 w0 = Wt[(tid >> 5) * P + p];
            float wr_ = w0.x, wi_ = w0.y;
#pragma unroll
            for (int j = 0; j < 8; j++) {
                float nr = lbr * wr_ - lbi * wi_, ni = lbr * wi_ + lbi * wr_; wr_ = nr; wi_ = ni;
                int r = strun + j;
                char* rowp = smem + cur * 16384 + r * 128;
                *(u16*)(rowp + ((((sp >> 3)) ^ (r & 7)) << 4) + (sp & 7) * 2) = f2bf(hv[cur][j].x + wr_);
                *(u16*)(rowp + (((4 + (sp >> 3)) ^ (r & 7)) << 4) + (sp & 7) * 2) = f2bf(-(hv[cur][j].y + wi_));
            }
#pragma unroll
            for (int i = 0; i < 2; i++) *(u32x4*)(smem + cur * 16384 + cdst[i]) = cv[cur][i];
        }
        __syncthreads();
#pragma unroll
        for (int kk = 0; kk < 4; kk++) {
            int ch = 2 * kk + lh; int brow = wc * 32 + l31;
            s16x8 af = *(const s16x8*)(smem + cur * 16384 + arow * 128 + ((ch ^ (arow & 7)) << 4));
            s16x8 bf = *(const s16x8*)(smem + cur * 16384 + 8192 + brow * 128 + ((ch ^ (brow & 7)) << 4));
            accy = MFMA32(af, bf, accy);
        }
    }
    const int d = d0 + wc * 32 + l31;
    const float dsk = bf2f(Dsk[d]);
#pragma unroll
    for (int e = 0; e < 16; e++) {
        int t = t0 + wr * 32 + (e & 3) + 8 * (e >> 2) + 4 * lh;
        float dn = rbf(acc[e]);
        float g = rbf(dn / (1.0f + expf(-dn)));
        float sv = 2.0f * accy[e] + rbf(bf2f(xn[(size_t)t * D + d]) * dsk);
        out[(size_t)t * D + d] = bf2f(x[(size_t)t * D + d]) + sv * g;
    }
}

extern "C" void kernel_launch(void* const* d_in, const int* in_sizes, int n_in,
                              void* d_out, int out_size, void* d_ws, size_t ws_size,
                              hipStream_t stream) {
    const u16* x = (const u16*)d_in[0];
    const u16* Lre = (const u16*)d_in[1];
    const u16* Lim = (const u16*)d_in[2];
    const u16* Bre = (const u16*)d_in[3];
    const u16* Bim = (const u16*)d_in[4];
    const u16* Cre = (const u16*)d_in[5];
    const u16* Cim = (const u16*)d_in[6];
    const u16* logdt = (const u16*)d_in[7];
    const u16* Dsk = (const u16*)d_in[8];
    const u16* wg = (const u16*)d_in[10];
    const u16* wu = (const u16*)d_in[11];
    const u16* wd = (const u16*)d_in[12];
    char* ws = (char*)d_ws;
    u16* xn = (u16*)ws;
    float2* hloc = (float2*)(ws + (4 << 20));
    u16* h = (u16*)(ws + (16 << 20));
    float2* Ec = (float2*)(ws + (24 << 20));
    ln_k2<<<512, 256, 0, stream>>>(x, xn);
    k1<<<256, 256, 0, stream>>>(x, Lre, Lim, Bre, Bim, logdt, wg, wu, xn, h, hloc, Ec);
    k2<<<256, 256, 0, stream>>>(x, xn, h, hloc, Ec, Lre, Lim, logdt, Cre, Cim, wd, Dsk, (float*)d_out);
}
```
